```python
import torch

def _softmax(x, axis=-1):
    return torch.softmax(x, dim=axis)
def _gelu(x):
    return torch.nn.functional.gelu(x, approximate="tanh")
def _silu(x):
    return torch.nn.functional.silu(x)
def _sigmoid(x):
    return torch.sigmoid(x)
def _layernorm(x, axis=-1, eps=1e-5):
    m = x.mean(dim=axis, keepdim=True)
    v = x.var(dim=axis, keepdim=True, unbiased=False)
    return (x - m) / torch.sqrt(v + eps)
def _scatter_add(tgt, idx, src, axis=0):
    return tgt.clone().index_add_(axis, idx, src)
def _segment_sum(src, seg, n):
    out = torch.zeros((n,) + src.shape[1:], dtype=src.dtype)
    return out.index_add_(0, seg, src)
def _pad(x, pw):
    flat = tuple(v for pair in reversed(pw) for v in pair)
    return torch.nn.functional.pad(x, flat)

kernel_name = 'recurrent_scan__compressed_kv_cla__geglu_ffn__decode'

def setup_inputs():

    _w = (lambda a: (lambda t: t.to(torch.bfloat16) if t.is_floating_point() else t)(torch.as_tensor(a)))
    def _A_setup():
        B, H, Sq, Sk, D, M = 1, 32, 1, 16384, 128, 512
        Dc = 4 * D
        return {
            "x": _w((lambda *s: torch.randn(*s, dtype=torch.bfloat16) * 0.05)(B, Sq, M)),
            "c_kv": _w((lambda *s: torch.randn(*s, dtype=torch.bfloat16) * 0.8)(B, Sk, Dc)),
            "w_q": _w((lambda *s: torch.randn(*s, dtype=torch.bfloat16) * 1.6)(M, H, D) * (M ** -0.5)),
            "w_uk": _w((lambda *s: torch.randn(*s, dtype=torch.bfloat16) * 1.6)(Dc, H, D) * (Dc ** -0.5)),
            "w_uv": _w((lambda *s: torch.randn(*s, dtype=torch.bfloat16) * 0.8)(Dc, H, D) * (Dc ** -0.5)),
            "w_o": _w((lambda *s: torch.randn(*s, dtype=torch.bfloat16) * 0.8)(H, D, M) * ((H * D) ** -0.5)),
        }
    _A_in = _A_setup()

    def _B_setup():
        B, S, D, F = 1, 1, 512, 1792
        return {
            "x": _w((lambda *s: torch.randn(*s, dtype=torch.bfloat16) * 0.05)(B, S, D)),
            "w_gate": _w((lambda *s: torch.randn(*s, dtype=torch.bfloat16) * 0.2)(D, F)),
            "w_up": _w((lambda *s: torch.randn(*s, dtype=torch.bfloat16) * 0.2)(D, F)),
            "w_down": _w((lambda *s: torch.randn(*s, dtype=torch.bfloat16) * 0.2)(F, D)),
        }
    _B_in = _B_setup()

    return {
        "A_x": _A_in["x"],
        "A_c_kv": _A_in["c_kv"],
        "A_w_q": _A_in["w_q"],
        "A_w_uk": _A_in["w_uk"],
        "A_w_uv": _A_in["w_uv"],
        "A_w_o": _A_in["w_o"],
        "B_x": _B_in["x"],
        "B_w_gate": _B_in["w_gate"],
        "B_w_up": _B_in["w_up"],
        "B_w_down": _B_in["w_down"],
    }

def reference(A_x, A_c_kv, A_w_q, A_w_uk, A_w_uv, A_w_o, B_x, B_w_gate, B_w_up, B_w_down):
    def _A_ref(x, c_kv, w_q, w_uk, w_uv, w_o):
        B, Sq, M = x.shape[0], x.shape[1], x.shape[2]
        Sk = c_kv.shape[1]
        H, D = w_q.shape[1], w_q.shape[2]
        q = torch.einsum("bqm,mhd->bhqd", x, w_q)
        k = torch.einsum("bkc,chd->bhkd", c_kv, w_uk)
        v = torch.einsum("bkc,chd->bhkd", c_kv, w_uv)
        scores = torch.einsum("bhqd,bhkd->bhqk", q, k) * (D ** -0.5)
        off = Sk - Sq
        qi = torch.arange(Sq)[:, None]
        kj = torch.arange(Sk)[None, :]
        cmask = kj <= (qi + off)
        scores = torch.where(cmask[None, None], scores, -1e9)
        p = _softmax(scores, axis=-1)
        ctx = torch.einsum("bhqk,bhkd->bhqd", p, v)
        return torch.einsum("bhqd,hdm->bqm", ctx, w_o)

    def _B_ref(x, w_gate, w_up, w_down):
        return (_gelu(x @ w_gate) * (x @ w_up)) @ w_down

    x = A_x
    xn = _layernorm(x, axis=-1)
    s = _A_ref(xn, A_c_kv, A_w_q, A_w_uk, A_w_uv, A_w_o)
    g = _silu(_B_ref(xn, B_w_gate, B_w_up, B_w_down))
    return x + s * g
```

```cpp
#include <hip/hip_runtime.h>
#include <hip/hip_bf16.h>
typedef unsigned short u16;
__device__ __forceinline__ float bf2f(u16 b){ return __uint_as_float(((unsigned)b)<<16); }
__device__ __forceinline__ u16 f2bf(float f){ unsigned u=__float_as_uint(f); u += 0x7fffu + ((u>>16)&1u); return (u16)(u>>16); }
constexpr int H=32, SK=16384, D=128, M=512, DC=512, F=1792;

__device__ void ln(const u16* x, float* xn, float* red){
  int t=threadIdx.x;
  float a=bf2f(x[t]), b=bf2f(x[t+256]);
  float s=a+b;
  for(int o=32;o;o>>=1) s+=__shfl_xor(s,o);
  if((t&63)==0) red[t>>6]=s;
  __syncthreads();
  float mean=(red[0]+red[1]+red[2]+red[3])*(1.f/512.f);
  __syncthreads();
  float da=a-mean, db=b-mean;
  s=da*da+db*db;
  for(int o=32;o;o>>=1) s+=__shfl_xor(s,o);
  if((t&63)==0) red[t>>6]=s;
  __syncthreads();
  float var=(red[0]+red[1]+red[2]+red[3])*(1.f/512.f);
  float r=1.f/sqrtf(var+1e-5f);
  xn[t]=da*r; xn[t+256]=db*r;
  __syncthreads();
}
__global__ void __launch_bounds__(256) k1(const u16* x,const u16* wq,const u16* wuk,const u16* wg,const u16* wu,float* qabs,float* hid){
  __shared__ float xn[512]; __shared__ float red[4]; __shared__ float q[256];
  ln(x,xn,red);
  int t=threadIdx.x;
  if(blockIdx.x<256){
    int h=blockIdx.x>>3, j=blockIdx.x&7;
    int d=t&127, half=t>>7;
    float acc=0;
    for(int m=half*256;m<half*256+256;m++) acc+=xn[m]*bf2f(wq[(size_t)m*4096+h*128+d]);
    q[t]=acc; __syncthreads();
    if(t<128) q[t]=q[t]+q[t+128];
    __syncthreads();
    int c=j*64+(t>>2), p=t&3;
    float a=0;
    for(int dd=p*32;dd<p*32+32;dd++) a+=q[dd]*bf2f(wuk[(size_t)c*4096+h*128+dd]);
    a+=__shfl_xor(a,1); a+=__shfl_xor(a,2);
    if(p==0) qabs[h*512+c]=a*0.08838834764831845f;
  } else {
    int f=(blockIdx.x-256)*64+(t&63), part=t>>6;
    float g=0,u=0;
    for(int m=part*128;m<part*128+128;m++){ g+=xn[m]*bf2f(wg[(size_t)m*F+f]); u+=xn[m]*bf2f(wu[(size_t)m*F+f]); }
    __shared__ float gs[256],us[256];
    gs[t]=g; us[t]=u; __syncthreads();
    if(t<64){ g=gs[t]+gs[t+64]+gs[t+128]+gs[t+192]; u=us[t]+us[t+64]+us[t+128]+us[t+192];
      float ge=0.5f*g*(1.f+tanhf(0.7978845608028654f*(g+0.044715f*g*g*g)));
      hid[f]=ge*u; }
  }
}
__global__ void __launch_bounds__(256) k2(const u16* ckv,const float* qabs,float* part,float* ml){
  __shared__ u16 kv[64*520]; __shared__ float p[32*64];
  int t=threadIdx.x, s=blockIdx.x;
  for(int i=t;i<64*64;i+=256){ int r=i>>6,c=i&63; *(uint4*)&kv[r*520+c*8]=*(const uint4*)&ckv[(size_t)(s*64+r)*512+c*8]; }
  __syncthreads();
  int k=t&63, hg=t>>6;
  for(int hh=0;hh<8;hh++){ int h=hg*8+hh; float a=0; _Pragma("unroll 4") for(int c=0;c<512;c++) a+=qabs[h*512+c]*bf2f(kv[k*520+c]);
    float m=a; for(int o=32;o;o>>=1) m=fmaxf(m,__shfl_xor(m,o));
    float e=__expf(a-m); float l=e; for(int o=32;o;o>>=1) l+=__shfl_xor(l,o);
    p[h*64+k]=e; if(k==0){ ml[(h*256+s)*2]=m; ml[(h*256+s)*2+1]=l; } }
  __syncthreads();
  for(int h=0;h<32;h++){ float a0=0,a1=0; _Pragma("unroll 4") for(int kk=0;kk<64;kk++){ float pp=p[h*64+kk]; a0+=pp*bf2f(kv[kk*520+t]); a1+=pp*bf2f(kv[kk*520+t+256]); }
    part[((size_t)h*256+s)*512+t]=a0; part[((size_t)h*256+s)*512+t+256]=a1; }
}
__global__ void __launch_bounds__(256) k3(const float* part,const float* ml,const u16* wuv,const u16* wo,const float* hid,const u16* wd,float* sp,float* fp){
  int t=threadIdx.x;
  if(blockIdx.x<256){
    int h=blockIdx.x>>3,j=blockIdx.x&7;
    __shared__ float w[256]; __shared__ float red[8]; __shared__ float cl[256]; __shared__ float ctx[256];
    float m=ml[(h*256+t)*2], l=ml[(h*256+t)*2+1];
    float mm=m; for(int o=32;o;o>>=1) mm=fmaxf(mm,__shfl_xor(mm,o));
    if((t&63)==0) red[t>>6]=mm; __syncthreads();
    mm=fmaxf(fmaxf(red[0],red[1]),fmaxf(red[2],red[3]));
    float e=__expf(m-mm); float s=e*l; for(int o=32;o;o>>=1) s+=__shfl_xor(s,o);
    if((t&63)==0) red[4+(t>>6)]=s; __syncthreads();
    s=red[4]+red[5]+red[6]+red[7];
    w[t]=e/s; __syncthreads();
    int c=t&63,g=t>>6; float a=0;
    for(int ss=g*64;ss<g*64+64;ss++) a+=w[ss]*part[((size_t)h*256+ss)*512+j*64+c];
    cl[t]=a; __syncthreads();
    if(t<64) cl[t]=cl[t]+cl[t+64]+cl[t+128]+cl[t+192];
    __syncthreads();
    int d=t&127,hf=t>>7; a=0;
    for(int cc=hf*32;cc<hf*32+32;cc++) a+=cl[cc]*bf2f(wuv[(size_t)(j*64+cc)*4096+h*128+d]);
    ctx[t]=a; __syncthreads();
    float a0=0,a1=0;
    for(int dd=0;dd<128;dd++){ float cv=ctx[dd]+ctx[dd+128]; a0+=cv*bf2f(wo[(size_t)(h*128+dd)*512+t]); a1+=cv*bf2f(wo[(size_t)(h*128+dd)*512+t+256]); }
    sp[blockIdx.x*512+t]=a0; sp[blockIdx.x*512+t+256]=a1;
  } else {
    int b=blockIdx.x-256; float a0=0,a1=0;
    for(int f=b*64;f<b*64+64;f++){ float hv=hid[f]; a0+=hv*bf2f(wd[(size_t)f*512+t]); a1+=hv*bf2f(wd[(size_t)f*512+t+256]); }
    fp[b*512+t]=a0; fp[b*512+t+256]=a1;
  }
}
__global__ void __launch_bounds__(256) k4(const u16* x,const float* sp,const float* fp,u16* out){
  int t=threadIdx.x; int col=blockIdx.x*16+(t&15), g=t>>4;
  float s=0,f=0;
  for(int i=g;i<256;i+=16) s+=sp[i*512+col];
  for(int i=g;i<28;i+=16) f+=fp[i*512+col];
  __shared__ float ss[256],fs[256]; ss[t]=s; fs[t]=f; __syncthreads();
  if(t<16){ s=0;f=0; for(int i=0;i<16;i++){ s+=ss[t+16*i]; f+=fs[t+16*i]; }
    float g2=f/(1.f+__expf(-f)); out[col]=f2bf(bf2f(x[col])+s*g2); }
}
extern "C" void kernel_launch(void* const* d_in, const int* in_sizes, int n_in, void* d_out, int out_size, void* d_ws, size_t ws_size, hipStream_t stream){
  const u16* x=(const u16*)d_in[0]; const u16* ckv=(const u16*)d_in[1]; const u16* wq=(const u16*)d_in[2]; const u16* wuk=(const u16*)d_in[3];
  const u16* wuv=(const u16*)d_in[4]; const u16* wo=(const u16*)d_in[5]; const u16* wg=(const u16*)d_in[7]; const u16* wu=(const u16*)d_in[8]; const u16* wd=(const u16*)d_in[9];
  float* ws=(float*)d_ws;
  float* qabs=ws; float* hid=ws+16384; float* ml=ws+32768; float* sp=ws+65536; float* fp=sp+256*512; float* part=fp+32*512;
  k1<<<256+28,256,0,stream>>>(x,wq,wuk,wg,wu,qabs,hid);
  k2<<<256,256,0,stream>>>(ckv,qabs,part,ml);
  k3<<<256+28,256,0,stream>>>(part,ml,wuv,wo,hid,wd,sp,fp);
  k4<<<32,256,0,stream>>>(x,sp,fp,(u16*)d_out);
}
```

```cpp
#define CODE_BYTES 23680
#define POL 0
#define REPF 2
#define D_DELAY 120
#define X2_FIRST 370
#define TAIL_AT 400
#define UK_DELAY 40
#define X1_WAIT 45
#define FF_AT 235
#define UV_OFF 0
#define XF_WAIT 60
#define ML_SLEEP 0
#define ML_AT 580
#define XS_AT 140
#define HELP 4
#include <hip/hip_runtime.h>

typedef unsigned short u16;
typedef unsigned long long u64;
typedef unsigned v4u __attribute__((ext_vector_type(4)));
typedef unsigned v2u __attribute__((ext_vector_type(2)));
typedef float f32x2 __attribute__((ext_vector_type(2)));
typedef float f32x4 __attribute__((ext_vector_type(4)));
typedef float f32x16 __attribute__((ext_vector_type(16)));
typedef __bf16 bf16x2 __attribute__((ext_vector_type(2)));
typedef __bf16 bf16x8 __attribute__((ext_vector_type(8)));
typedef short s16x4 __attribute__((ext_vector_type(4)));
#define GAS __attribute__((address_space(1)))
#define LAS __attribute__((address_space(3)))
#define DI __device__ __forceinline__

#define KARG
#define KPASS
#define INFL 10
#define TRB2 11
#define TRB3 19
#define B5POS 26
#define FF_SLEEP 12
#define DW_MODE 4
#define WO_FIRST 0
#define MLS 0
#define PRE 45
#define HELP_END 900
#define XG_LATE 0
#define UVPOS 3
#define X2_EARLY 0
#define UKPF 2
#define CLR_AT 90
#define RDY_AT 165
#define NTS ""
#define NTA 0
#define WSOFF 0
#define HMIX 1
#define HI_SLEEP 3
#define HI_POS 15
#define HINFL 8
#define PGMODE 0
#define PAGE(k) (PGMODE == 0 ? ((k) * 4 + w) * 128 + s : PGMODE == 1 ? s * 32 + (k) * 4 + w : (s & 7) * 512 + ((k) * 4 + w) * 16 + (s >> 3))
constexpr int WS_READY = 8192, WS_X2 = 32768, WS_X1 = 65536, WS_ML = 262144, WS_FL = 393216, WS_FB = 786432, WS_HT = 917504, WS_XS = 524288, WS_XF = 1048576, WS_XG = 1572864,
              WS_X3 = 2097152, WS_BYTES = 6291456, WS_STAMP = 8388608;
constexpr int SC1 = 16;
constexpr int L_Q = 131072, L_P = L_Q, L_MX = L_Q + 8192, L_M2 = L_MX + 512, L_LS = L_MX + 1024, L_FIN = L_Q + 12288;
constexpr unsigned SPIN_CAP = 1u << 16;

DI unsigned rt() { return (unsigned)__builtin_amdgcn_s_memrealtime(); }
DI void wait_until(unsigned t) { while ((int)(rt() - t) < 0) __builtin_amdgcn_s_sleep(1); }
DI unsigned pk_bf16(float a, float b) { f32x2 v = {a, b}; return __builtin_bit_cast(unsigned, __builtin_convertvector(v, bf16x2)); }
DI float lo_f(unsigned p) { return __uint_as_float(p << 16); }
DI float hi_f(unsigned p) { return __uint_as_float(p & 0xffff0000u); }
DI float rbf(float a) { return lo_f(pk_bf16(a, a)); }
DI v4u ldnt(const void* p) { return __builtin_nontemporal_load((const GAS v4u*)p); }
DI f32x4 mfma16(v4u a, v4u b, f32x4 c) { return __builtin_amdgcn_mfma_f32_16x16x32_bf16(__builtin_bit_cast(bf16x8, a), __builtin_bit_cast(bf16x8, b), c, 0, 0, 0); }
DI float dot2(unsigned a, unsigned b, float c) { return __builtin_amdgcn_fdot2_f32_bf16(__builtin_bit_cast(bf16x2, a), __builtin_bit_cast(bf16x2, b), c, false); }
DI float fmx(float a, float b) { return __builtin_elementwise_maximum(a, b); }
template <class T> DI LAS T* lp(unsigned a) { return (LAS T*)a; }
template <int CTRL> DI float dpp(float v) { return __int_as_float(__builtin_amdgcn_update_dpp(0, __float_as_int(v), CTRL, 0xf, 0xf, true)); }
DI float xrow_sum(float v) {
  auto a = __builtin_amdgcn_permlane16_swap(__float_as_uint(v), __float_as_uint(v), false, false);
  v = __uint_as_float(a[0]) + __uint_as_float(a[1]);
  auto b = __builtin_amdgcn_permlane32_swap(__float_as_uint(v), __float_as_uint(v), false, false);
  return __uint_as_float(b[0]) + __uint_as_float(b[1]);
}
DI float xrow_max(float v) {
  auto a = __builtin_amdgcn_permlane16_swap(__float_as_uint(v), __float_as_uint(v), false, false);
  v = fmx(__uint_as_float(a[0]), __uint_as_float(a[1]));
  auto b = __builtin_amdgcn_permlane32_swap(__float_as_uint(v), __float_as_uint(v), false, false);
  return fmx(__uint_as_float(b[0]), __uint_as_float(b[1]));
}
DI float wave_sum(float v) { v += dpp<0x121>(v); v += dpp<0x122>(v); v += dpp<0x124>(v); v += dpp<0x128>(v); return xrow_sum(v); }
DI float wave_max(float v) { v = fmx(v, dpp<0x121>(v)); v = fmx(v, dpp<0x122>(v)); v = fmx(v, dpp<0x124>(v)); v = fmx(v, dpp<0x128>(v)); return xrow_max(v); }
#define BAR() do { asm volatile("s_waitcnt lgkmcnt(0)" ::: "memory"); __builtin_amdgcn_s_barrier(); asm volatile("" ::: "memory"); } while (0)
#define VMWAIT(n) asm volatile("s_waitcnt vmcnt(%0)" ::"i"(n) : "memory")
#define MF(acc, a, b) asm volatile("v_mfma_f32_16x16x32_bf16 %0, %1, %2, %0" : "+v"(acc) : "v"(a), "v"(b))
#define MF0(acc, a, b) asm volatile("v_mfma_f32_16x16x32_bf16 %0, %1, %2, 0" : "=&v"(acc) : "v"(a), "v"(b))
#define MG(acc, a, b) asm volatile("v_mfma_f32_32x32x16_bf16 %0, %1, %2, %0" : "+v"(acc) : "v"(a), "v"(b))
#define MG0(acc, a, b) asm volatile("v_mfma_f32_32x32x16_bf16 %0, %1, %2, 0" : "=&v"(acc) : "v"(a), "v"(b))
#define SB() __builtin_amdgcn_sched_barrier(0)
#define PAGE_START() asm volatile("s_branch 1f\n\t.p2align 12\n1:" ::: "memory")
#define FENCE() asm volatile("" ::: "memory")
#define POLL_BEGIN() { unsigned sp_ = SPIN_CAP; bool more_; _Pragma("nounroll") do { FENCE(); bool ok = true;
#define POLL_END(slp) more_ = __builtin_amdgcn_ballot_w64(!ok) != 0; POLL_COUNT(); if (slp && more_) __builtin_amdgcn_s_sleep(slp); } while (more_ && --sp_); FENCE(); }

#define STAMP(i)
#define POLL_COUNT()
#define STAMP_OUT()

DI void ln_stats(v4u xf, float& mean, float& rden) {
  float a[8] = {lo_f(xf.x), hi_f(xf.x), lo_f(xf.y), hi_f(xf.y), lo_f(xf.z), hi_f(xf.z), lo_f(xf.w), hi_f(xf.w)};
  float s = ((a[0] + a[1]) + (a[2] + a[3])) + ((a[4] + a[5]) + (a[6] + a[7]));
  const float mu = wave_sum(s) * (1.0f / 512.0f);
  float q = 0.f;
#pragma unroll
  for (int i = 0; i < 8; ++i) { const float d = a[i] - mu; q = fmaf(d, d, q); }
  const float var = rbf(wave_sum(q) * (1.0f / 512.0f));
  const float den = rbf(__builtin_amdgcn_sqrtf(rbf(var + 1e-5f)));
  mean = rbf(mu);
  rden = __builtin_amdgcn_rcpf(den);
}
DI unsigned ln_pair(unsigned xp, float mean, float den) {
  const unsigned d = pk_bf16(lo_f(xp) - mean, hi_f(xp) - mean);
  return pk_bf16(lo_f(d) * den, hi_f(d) * den);
}
DI v4u sel_frag(unsigned dup, v4u mask) { return v4u{dup & mask.x, dup & mask.y, dup & mask.z, dup & mask.w}; }
DI unsigned dup_lo(unsigned p) { return __builtin_amdgcn_perm(p, p, 0x01000100u); }
DI unsigned dup_hi(unsigned p) { return __builtin_amdgcn_perm(p, p, 0x03020302u); }
template <int NT> DI void dma16(unsigned lds, unsigned voff, v4u srd, unsigned soff) {
  if (NT) asm volatile("s_mov_b32 m0, %0\n\ts_nop 0\n\tbuffer_load_dwordx4 %1, %2, %3 offen nt lds" ::"s"(lds), "v"(voff), "s"(srd), "s"(soff) : "memory");
  else asm volatile("s_mov_b32 m0, %0\n\ts_nop 0\n\tbuffer_load_dwordx4 %1, %2, %3 offen lds" ::"s"(lds), "v"(voff), "s"(srd), "s"(soff) : "memory");
}
DI v4u make_srd(const void* p, unsigned bytes) { return v4u{(unsigned)(u64)p, (unsigned)((u64)p >> 32) & 0xffffu, bytes, 0x00020000u}; }
DI void transpose8(const v4u (&r)[8], v4u (&f)[8]) {
#pragma unroll
  for (int c = 0; c < 4; ++c)
#pragma unroll
    for (int jd = 0; jd < 4; ++jd) {
      f[2 * c][jd] = __builtin_amdgcn_perm(r[2 * jd + 1][c], r[2 * jd][c], 0x05040100u);
      f[2 * c + 1][jd] = __builtin_amdgcn_perm(r[2 * jd + 1][c], r[2 * jd][c], 0x07060302u);
    }
}

__global__ void __launch_bounds__(512, 2) __attribute__((aligned(4096)))
fused_decode(const u16* __restrict__ x, const u16* __restrict__ ckv, const u16* __restrict__ wq, const u16* __restrict__ wuk,
             const u16* __restrict__ wuv, const u16* __restrict__ wo, const u16* __restrict__ wg, const u16* __restrict__ wu,
             const u16* __restrict__ wd, u16* __restrict__ out, unsigned char* __restrict__ ws KARG) {
  extern __shared__ __attribute__((aligned(16))) unsigned char smem[];
  const unsigned lb = (unsigned)(size_t)(LAS unsigned char*)smem;
  u64 pc0;
  asm volatile("s_getpc_b64 %0" : "=s"(pc0));
  asm volatile("" ::"s"(x), "s"(ckv), "s"(wq), "s"(wuk), "s"(wuv), "s"(wo), "s"(wg), "s"(wu), "s"(wd), "s"(out), "s"(ws));
  const unsigned t0 = rt();
  const int tid = threadIdx.x, lane = tid & 63, w = __builtin_amdgcn_readfirstlane(tid >> 6);
  const int n = lane & 15, g = lane >> 4;
  const int xc = blockIdx.x & 7, r = blockIdx.x >> 3;
  const __amdgpu_buffer_rsrc_t rs = __builtin_amdgcn_make_buffer_rsrc((void*)ws, 0, WS_BYTES, 0x00020000);
  const v4u ones = {~0u, ~0u, ~0u, ~0u};
  auto get_tag = [&](v2u e) {
    u64 z = (((u64)(unsigned)__builtin_amdgcn_readfirstlane(e.y) << 32) | (unsigned)__builtin_amdgcn_readfirstlane(e.x)) + 0x9E3779B97F4A7C15ull;
    z = (z ^ (z >> 30)) * 0xBF58476D1CE4E5B9ull;
    z = (z ^ (z >> 27)) * 0x94D049BB133111EBull;
    z ^= z >> 31;
    return v2u{(unsigned)z, (unsigned)(z >> 32)};
  };
  int bx = blockIdx.x;
  asm volatile("" : "+s"(bx));
  if (bx >= 128) {
    if (w >= 5) {
      const __amdgpu_buffer_rsrc_t rc = __builtin_amdgcn_make_buffer_rsrc((void*)pc0, 0, CODE_BYTES, 0x00020000);
      const v4u c = __builtin_amdgcn_raw_buffer_load_b128(rc, (r - 16 + 16 * (w - 5)) * 1024 + lane * 16, 0, 0);
      asm volatile("" ::"v"(c));
    }
    if (bx < 136 && w == 4) {
      const v2u ep = __builtin_amdgcn_raw_buffer_load_b64(rs, 0, 0, SC1);
#pragma unroll
      for (int i = 0; i < 4; ++i) __builtin_amdgcn_raw_buffer_store_b128(ones, rs, WS_X2 + xc * 4096 + i * 1024 + lane * 16, 0, SC1);
      const v2u tag = get_tag(ep);
      VMWAIT(0);
      __builtin_amdgcn_raw_buffer_store_b128(v4u{t0, tag.x, t0, tag.y}, rs, WS_READY + ((lane >> 3) * 8 + xc) * 128 + (lane & 7) * 16, 0, SC1);
    }
  }
  auto get_mask = [&]() {
    int nn = n;
    asm volatile("" : "+v"(nn));
    const unsigned hm = (nn & 1) ? 0xffff0000u : 0x0000ffffu;
    const int jd = (nn & 7) >> 1;
    return v4u{jd == 0 ? hm : 0u, jd == 1 ? hm : 0u, jd == 2 ? hm : 0u, jd == 3 ? hm : 0u};
  };
  auto wait_ready = [&](v2u tag) {
    v4u v;
    POLL_BEGIN()
      v = __builtin_amdgcn_raw_buffer_load_b128(rs, WS_READY + (xc * 8 + (lane & 7)) * 128, 0, SC1);
      ok = (v.y == tag.x) & (v.w == tag.y);
    POLL_END(1)
    int d = min(max((int)(v.x - t0), -200), 200);
    d = max(d, __builtin_amdgcn_update_dpp(d, d, 0x121, 0xf, 0xf, false));
    d = max(d, __builtin_amdgcn_update_dpp(d, d, 0x122, 0xf, 0xf, false));
    d = max(d, __builtin_amdgcn_update_dpp(d, d, 0x124, 0xf, 0xf, false));
    return t0 + (unsigned)__builtin_amdgcn_readfirstlane(d);
  };
  auto full = [&](v4u v) { return max(max(max(v.x, v.y), v.z), v.w) != ~0u; };
  auto full4 = [&](const v4u (&v)[4]) {
    unsigned m = 0;
#pragma unroll
    for (int i = 0; i < 4; ++i) m = max(max(max(max(m, v[i].x), v[i].y), v[i].z), v[i].w);
    return m != ~0u;
  };
  auto dma_voff = [&](int k, int pitch) { return (unsigned)(g * pitch + 16 * ((n ^ (2 * g)) ^ (8 * (k & 1) | (k >> 1)))); };

  if (r >= 16) {
    const int rr = r - 16, s = rr * 8 + xc;
    const bool ffn = xc != 0;
    const int fu = rr * 7 + xc - 1, fcb = fu >> 3, fe = fu & 7;
    constexpr float LOG2E = 1.4426950408889634f, SC2 = 0.08838834764831845f * LOG2E;
    if (w >= 4) {
      const int lw = w - 4;
      v2u ep;
      asm volatile("global_load_dwordx2 %0, %1, %2 sc1" : "=&v"(ep) : "v"(0), "s"(ws) : "memory");
      if (UKPF == 2) {
        wait_until(t0 + UK_DELAY);
        const v4u su = make_srd(wuk, 512u * 8192u);
#pragma unroll
        for (int i = 0; i < 8; ++i) dma16<POL & 4>(lb + L_Q + lw * 8192, g * 8192 + n * 16, su, (128 * (rr & 3) + 32 * lw + 4 * i) * 8192 + (xc + 8 * (rr >> 2)) * 256);
      }
      __builtin_amdgcn_s_setprio(3);
      asm volatile("s_waitcnt vmcnt(%1)" : "+v"(ep) : "i"(UKPF == 2 ? 8 : 0) : "memory");
      const v2u tag = get_tag(ep);
      wait_until(t0 + CLR_AT);
      __builtin_amdgcn_raw_buffer_store_b128(ones, rs, (lane < 32 ? WS_XS + s * 2048 : ffn ? WS_XF + fu * 2048 : 0x7ffff000) + lw * 512 + (lane & 31) * 16, 0, SC1);
      wait_until(t0 + RDY_AT);
      const unsigned tr = wait_ready(tag);
      FENCE();
      STAMP(1);
      unsigned ta[8];
      {
        const int r8 = 4 * (g & 1) + (n >> 2), p = n & 3;
#pragma unroll
        for (int t = 0; t < 8; ++t) {
          ta[t] = lb + lw * 32768 + (16 * (g >> 1) + r8) * 256 + 8 * (p & 1) + 16 * ((2 * t + (p >> 1)) ^ (r8 << 1));
          asm volatile("" : "+v"(ta[t]));
        }
      }
      int so = WS_X3 + (4 * g * 4 + lw) * 32768 + s * 256 + n * 16;
      unsigned pra = lb + L_P + lane * 16, qw[4];
#pragma unroll
      for (int i = 0; i < 4; ++i) qw[i] = lb + L_Q + lw * 8192 + i * 1024 + dma_voff(i, 256);
      asm volatile("" : "+v"(so), "+v"(pra), "+v"(qw[0]), "+v"(qw[1]), "+v"(qw[2]), "+v"(qw[3]));
      PAGE_START();
      wait_until(tr + X2_FIRST);
      STAMP(2);
      v4u kb[4][8];
      {
        const int go = WS_X2 + g * 1024 + lw * 256 + n * 16;
        v4u v[8];
        auto stage = [&]() {
          bool need[8], any = false;
#pragma unroll
          for (int i = 0; i < 8; ++i) {
            *lp<v4u>(qw[i & 3] + (i >> 2) * 4096) = v[i];
            need[i] = !full(v[i]);
            any |= need[i];
          }
          if (__builtin_expect(__builtin_amdgcn_ballot_w64(any) == 0, 1)) return true;
          FENCE();
#pragma unroll
          for (int i = 0; i < 8; ++i)
            if (need[i]) v[i] = __builtin_amdgcn_raw_buffer_load_b128(rs, go, i * 4096, SC1);
          return false;
        };
#pragma unroll
        for (int i = 0; i < 8; ++i) v[i] = __builtin_amdgcn_raw_buffer_load_b128(rs, go, i * 4096, SC1);
        v4u cd;
        if (REPF) {
          const __amdgpu_buffer_rsrc_t rc = __builtin_amdgcn_make_buffer_rsrc((void*)pc0, 0, CODE_BYTES, 0x00020000);
          cd = __builtin_amdgcn_raw_buffer_load_b128(rc, (rr + 16 * lw) * 1024 + lane * 16, 0, SC1);
        }
        bool done = false;
        if (X2_EARLY) done = stage();
        SB();
        FENCE(); __builtin_amdgcn_s_barrier(); FENCE();
#pragma unroll
        for (int k = 0; k < 32; ++k) {
          const v2u lo = __builtin_bit_cast(v2u, __builtin_amdgcn_ds_read_tr16_b64_v4i16(lp<s16x4>(ta[k & 7] + (k >> 3) * 8192)));
          kb[k >> 3][k & 7].x = lo.x; kb[k >> 3][k & 7].y = lo.y;
        }
        SB();
#pragma unroll
        for (int t = 0; t < 8; ++t) { ta[t] ^= 16; asm volatile("" : "+v"(ta[t])); }
        SB();
        if (!X2_EARLY) done = stage();
        if (REPF) asm volatile("" ::"v"(cd));
        _Pragma("nounroll") for (unsigned sp = SPIN_CAP; sp && !done; --sp) done = stage();
      }
      STAMP(3);
      __builtin_amdgcn_s_setprio(0);
      BAR();
      if (lw == 3) __builtin_amdgcn_raw_buffer_store_b128(v4u{rt(), tag.x, 0u, tag.y}, rs, WS_HT + s * 1024 + lane * 16, 0, SC1);
      STAMP(4);
      FENCE(); __builtin_amdgcn_s_barrier(); FENCE();
      __builtin_amdgcn_s_sleep(HI_SLEEP);
#pragma unroll
      for (int k = 0; k < 32; ++k) {
        const v2u hi = __builtin_bit_cast(v2u, __builtin_amdgcn_ds_read_tr16_b64_v4i16(lp<s16x4>(ta[k & 7] + (k >> 3) * 8192 + 2048)));
        kb[k >> 3][k & 7].z = hi.x; kb[k >> 3][k & 7].w = hi.y;
        if (k == HI_POS) { SB(); __builtin_amdgcn_s_barrier(); SB(); }
      }
      SB();
      f32x4 o[2][8];
      v4u pf[4], pg[4];
      __builtin_amdgcn_s_setprio(3);
      BAR();
#pragma unroll
      for (int u = 0; u < 4; ++u) pf[u] = *lp<v4u>(pra + u * 2048);
#pragma unroll
      for (int u = 0; u < 4; ++u) pg[u] = *lp<v4u>(pra + u * 2048 + 1024);
      STAMP(5);
      SB();
#pragma unroll
      for (int t = 0; t < 8; ++t) MF0(o[0][t], pf[0], kb[0][t]);
#pragma unroll
      for (int k = 8; k < 32; ++k) MF(o[0][k & 7], pf[k >> 3], kb[k >> 3][k & 7]);
      STAMP(6);
#pragma unroll
      for (int t = 0; t < 8; ++t) MF0(o[1][t], pg[0], kb[0][t]);
#pragma unroll
      for (int t = 0; t < 8; ++t) asm volatile("" : "+v"(o[0][t]));
      v4u sv[4];
#pragma unroll
      for (int k = 8; k < 32; ++k) {
        SB(); MF(o[1][k & 7], pg[k >> 3], kb[k >> 3][k & 7]); SB();
        const int e = (k - 8) / 5, ph = (k - 8) % 5;
        if (e < 4 && ph < 4) { unsigned c = pk_bf16(o[0][2 * ph][e], o[0][2 * ph + 1][e]); asm volatile("" : "+v"(c)); sv[e & 3][ph] = c; }
        if (e < 4 && ph == 4) __builtin_amdgcn_raw_buffer_store_b128(sv[e & 3], rs, so, e * 131072, SC1);
      }
      asm volatile("" ::"v"(sv[0]), "v"(sv[1]), "v"(sv[2]), "v"(sv[3]));
      asm volatile("s_nop 3" : "+v"(o[1][0]), "+v"(o[1][1]), "+v"(o[1][2]), "+v"(o[1][3]));
      v4u sw[4];
#pragma unroll
      for (int e = 0; e < 4; ++e) { sw[e].x = pk_bf16(o[1][0][e], o[1][1][e]); sw[e].y = pk_bf16(o[1][2][e], o[1][3][e]); }
      asm volatile("" : "+v"(sw[0].x), "+v"(sw[0].y), "+v"(sw[1].x), "+v"(sw[1].y), "+v"(sw[2].x), "+v"(sw[2].y), "+v"(sw[3].x), "+v"(sw[3].y),
                   "+v"(o[1][4]), "+v"(o[1][5]), "+v"(o[1][6]), "+v"(o[1][7]));
#pragma unroll
      for (int e = 0; e < 4; ++e) {
        sw[e].z = pk_bf16(o[1][4][e], o[1][5][e]); sw[e].w = pk_bf16(o[1][6][e], o[1][7][e]);
        __builtin_amdgcn_raw_buffer_store_b128(sw[e], rs, so, (16 + e) * 131072, SC1);
        SB();
      }
      asm volatile("s_nop 1" ::"v"(sw[0]), "v"(sw[1]), "v"(sw[2]), "v"(sw[3]));
      STAMP(8);
      FENCE(); __builtin_amdgcn_s_barrier();
      VMWAIT(0);
      STAMP(9);
      __builtin_amdgcn_s_barrier();
      STAMP_OUT();
    } else {
      v4u xf, xn;
      v2u ep;
      asm volatile("global_load_dwordx4 %0, %3, %5\n\tglobal_load_dwordx4 %1, %4, %5\n\tglobal_load_dwordx2 %2, %6, %7 sc1"
                   : "=&v"(xf), "=&v"(xn), "=&v"(ep) : "v"(lane * 16), "v"((64 * fe + 32 * (w >> 1) + 8 * g) * 2), "s"(x), "v"(0), "s"(ws) : "memory");
      const v4u srd = make_srd(ckv, 16384u * 1024u);
      const int r32 = lane & 31, hh = lane >> 5, f16 = ((n & 7) << 1) | (n >> 3);
      unsigned ka[8], qa[8];
#pragma unroll
      for (int st = 0; st < 8; ++st) {
        const unsigned fo = lb + r32 * 256 + 16 * ((2 * st + hh) ^ f16);
        ka[st] = fo + w * 8192;
        qa[st] = fo + L_Q;
        asm volatile("" : "+v"(ka[st]), "+v"(qa[st]));
      }
      unsigned mxa = lb + L_MX + r32 * 16 + w * 4, mra = lb + L_MX + r32 * 16, pwa = lb + L_P + w * 2048 + (r32 >> 4) * 1024 + (hh * 16 + n) * 16,
               lsa = lb + L_LS + r32 * 32 + (w * 2 + hh) * 4;
      int fvo = g * 8 * 3584 + n * 16, dvo = (4 * (g & 1) + 2 * (g >> 1)) * 1024 + n * 16;
      asm volatile("" : "+v"(dvo));
      asm volatile("" : "+v"(mxa), "+v"(mra), "+v"(pwa), "+v"(lsa), "+v"(fvo));
      wait_until(t0 + D_DELAY);
      STAMP(1);
      v4u dw[4];
      auto load_dw = [&]() {
        const __amdgpu_buffer_rsrc_t rd = __builtin_amdgcn_make_buffer_rsrc((void*)wd, 0, ffn ? 1792 * 1024 : 0, 0x00020000);
#pragma unroll
        for (int i = 0; i < 4; ++i) dw[i] = __builtin_amdgcn_raw_buffer_load_b128(rd, (4 * (g & 1) + 2 * (g >> 1)) * 1024 + n * 16, (fu * 16 + 8 * (i >> 1) + (i & 1)) * 1024 + w * 256, (POL & 128) ? 2 : 0);
      };
      if (DW_MODE == 0) load_dw();
      SB();
      _Pragma("nounroll") for (int k = 0; k < 8; ++k) {
        const int kr = (k & 1) | ((k & 2) << 1) | (k >> 2 << 1);
        const unsigned vo = dma_voff(kr & 3, 1024), so = PAGE(k) * 4096;
#pragma unroll
        for (int q = 0; q < 4; ++q) { VMWAIT(INFL - 1); dma16<POL & 1>(lb + q * 32768 + w * 8192 + kr * 1024, vo, srd, so + q * 256); }
      }
      static_assert(INFL <= 16, "the first sixteen keys must have landed");
      asm volatile("" : "+v"(xf), "+v"(xn), "+v"(ep));
      STAMP(2);
      FENCE(); __builtin_amdgcn_s_barrier(); FENCE();
      {
        float mean, den;
        ln_stats(xf, mean, den);
#pragma unroll
        for (int i = 0; i < 4; ++i) xn[i] = ln_pair(xn[i], mean, den);
        asm volatile("" : "+v"(xn));
      }
      const v2u tag = get_tag(ep);
      asm volatile("" ::"s"(tag.x), "s"(tag.y));
      SB();
      PAGE_START();
      VMWAIT(0);
      v4u kf[32];
#pragma unroll
      for (int k = 0; k < 16; ++k) kf[k] = *lp<v4u>(ka[k & 7] + (k >> 3) * 32768);
#pragma unroll
      for (int st = 0; st < 8; ++st) { ka[st] += 65536; asm volatile("" : "+v"(ka[st])); }
      SB();
      v4u fw[8];
      const __amdgpu_buffer_rsrc_t rf = __builtin_amdgcn_make_buffer_rsrc((void*)((w & 1) ? wu : wg), 0, ffn ? 512 * 3584 : 0, 0x00020000);
      int fso = (64 * fe + 32 * (w >> 1)) * 3584 + fcb * 256;
      int dso = fu * 16384 + w * 256;
      asm volatile("" : "+s"(fso));
      asm volatile("" : "+s"(dso));
      SB();
      STAMP(3);
      __builtin_amdgcn_s_setprio(3);
      FENCE(); __builtin_amdgcn_s_barrier(); FENCE();
      STAMP(4);
      f32x16 sc;
      v4u qf[8];
#define QRD(k) qf[(k) & 7] = *lp<v4u>(qa[(k) & 7] + ((k) >> 3) * 8192)
#pragma unroll
      for (int k = 0; k < 5; ++k) { QRD(k); SB(); }
#pragma unroll
      for (int k = 0; k < 32; ++k) {
        if (k == 0) MG0(sc, kf[0], qf[0]); else MG(sc, kf[k], qf[k & 7]);
        SB();
        if (k + 5 < 32) QRD(k + 5);
        if (k >= 8 && k < 24) kf[k + 8] = *lp<v4u>(ka[k & 7] + ((k - 8) >> 3) * 32768);
        if (k < 16 && (k & 1)) fw[k >> 1] = __builtin_amdgcn_raw_buffer_load_b128(rf, fvo, fso + (k >> 1) * 3584, (POL & 128) ? 2 : 0);
        if (k == 17) { FENCE(); __builtin_amdgcn_s_barrier(); FENCE(); }
        SB();
      }
      asm volatile("s_nop 11" : "+v"(sc));
      STAMP(5);
      float mx = fmx(fmx(sc[0], sc[1]), sc[2]), my = fmx(fmx(sc[3], sc[4]), sc[5]);
      mx = fmx(fmx(mx, sc[6]), sc[7]); my = fmx(fmx(my, sc[8]), sc[9]);
      mx = fmx(fmx(mx, sc[10]), sc[11]); my = fmx(fmx(my, sc[12]), sc[13]);
      mx = fmx(fmx(fmx(mx, sc[14]), sc[15]), my);
      {
        auto t = __builtin_amdgcn_permlane32_swap(__float_as_uint(mx), __float_as_uint(mx), false, false);
        *lp<float>(mxa) = fmx(__uint_as_float(t[0]), __uint_as_float(t[1]));
      }
      STAMP(6);
      BAR();
      const f32x4 m4 = *lp<f32x4>(mra);
      const float m2 = fmx(fmx(fmx(m4.x, m4.y), m4.z), m4.w) * SC2;
      v4u pp, pq;
#pragma unroll
      for (int i = 0; i < 4; ++i) {
        const f32x2 e = f32x2{sc[2 * i], sc[2 * i + 1]} * SC2 - m2;
        pp[i] = pk_bf16(__builtin_amdgcn_exp2f(e.x), __builtin_amdgcn_exp2f(e.y));
      }
      *lp<v4u>(pwa) = pp;
#pragma unroll
      for (int i = 0; i < 4; ++i) {
        const f32x2 e = f32x2{sc[8 + 2 * i], sc[9 + 2 * i]} * SC2 - m2;
        pq[i] = pk_bf16(__builtin_amdgcn_exp2f(e.x), __builtin_amdgcn_exp2f(e.y));
      }
      *lp<v4u>(pwa + 512) = pq;
      SB();
      STAMP(9);
      BAR();
      __builtin_amdgcn_s_setprio(0);
      __builtin_amdgcn_s_sleep(FF_SLEEP);
      asm volatile("" : "+v"(pp), "+v"(pq));
      *lp<float>(lsa) = dot2(pp.w, 0x3f803f80u, dot2(pp.z, 0x3f803f80u, dot2(pp.y, 0x3f803f80u, dot2(pp.x, 0x3f803f80u, 0.f)))) +
                        dot2(pq.w, 0x3f803f80u, dot2(pq.z, 0x3f803f80u, dot2(pq.y, 0x3f803f80u, dot2(pq.x, 0x3f803f80u, 0.f))));
      int mo = WS_ML + s * 1024 + (lane & 7) * 128 + (lane >> 3) * 16;
      asm volatile("" : "+v"(mo));
      auto flag = [&]() {
        v4u mlv;
        if (w == 0) {
          const f32x4 a = *lp<f32x4>(lb + L_LS + r32 * 32) + *lp<f32x4>(lb + L_LS + r32 * 32 + 16);
          mlv = v4u{__float_as_uint(m2), tag.x, __float_as_uint((a.x + a.y) + (a.z + a.w)), tag.y};
          asm volatile("" : "+v"(mlv));
        }
        FENCE(); __builtin_amdgcn_s_barrier(); FENCE();
        if (w == 0) __builtin_amdgcn_raw_buffer_store_b128(mlv, rs, mo, 0, SC1);
        SB();
      };
      if (ffn) {
        const v4u mask = get_mask();
        f32x4 acc = {0.f, 0.f, 0.f, 0.f};
        v4u af[8];
#pragma unroll
        for (int i = 0; i < 8; ++i) { af[i] = sel_frag((i & 1) ? dup_hi(xn[i >> 1]) : dup_lo(xn[i >> 1]), mask); asm volatile("" : "+v"(af[i])); }
        int go = WS_XG + ((fcb * 8 + (n >> 1)) * 16 + fe * 2 + (w >> 1)) * 256 + (w & 1) * 128 + (g * 2 + (n & 1)) * 16;
        asm volatile("" : "+v"(go));
#pragma unroll
        for (int i = 0; i < 8; ++i) acc = mfma16(af[i], fw[i], acc);
        {
          const float v0 = (g & 2) ? acc.z : acc.x, v1 = (g & 2) ? acc.w : acc.y;
          __builtin_amdgcn_raw_buffer_store_b128(v4u{__float_as_uint(v0), tag.x, __float_as_uint(v1), tag.y}, rs, go, 0, SC1);
        }
        STAMP(11);
        BAR();
        if (DW_MODE == 4) load_dw();
        SB();
        flag();
        STAMP(12);
        if (DW_MODE == 2) load_dw();
        wait_until(rt() + ML_SLEEP);
        v4u gv[4];
        POLL_BEGIN()
#pragma unroll
          for (int i = 0; i < 4; ++i) gv[i] = __builtin_amdgcn_raw_buffer_load_b128(rs, WS_XG + fu * 4096 + i * 1024 + lane * 16, 0, SC1);
#pragma unroll
          for (int i = 0; i < 4; ++i) ok &= (gv[i].y == tag.x) & (gv[i].w == tag.y);
        POLL_END(1)
        const float g0 = rbf(xrow_sum((__uint_as_float(gv[0].x) + __uint_as_float(gv[1].x)) + (__uint_as_float(gv[2].x) + __uint_as_float(gv[3].x))));
        const float g1 = rbf(xrow_sum((__uint_as_float(gv[0].z) + __uint_as_float(gv[1].z)) + (__uint_as_float(gv[2].z) + __uint_as_float(gv[3].z))));
        const float u0 = dpp<0x128>(g0), u1 = dpp<0x128>(g1);
        const float z0 = 0.7978845608028654f * (g0 + 0.044715f * g0 * g0 * g0), z1 = 0.7978845608028654f * (g1 + 0.044715f * g1 * g1 * g1);
        const float a0 = rbf(g0 * __builtin_amdgcn_rcpf(1.0f + __builtin_amdgcn_exp2f(-2.8853900817779268f * z0)));
        const float a1 = rbf(g1 * __builtin_amdgcn_rcpf(1.0f + __builtin_amdgcn_exp2f(-2.8853900817779268f * z1)));
        const unsigned hv = pk_bf16(a0 * u0, a1 * u1);
        const unsigned h0 = __builtin_amdgcn_ds_bpermute(8 * g, (int)hv), h1 = __builtin_amdgcn_ds_bpermute(8 * g + 4, (int)hv);
        acc = mfma16(sel_frag(dup_lo(h0), mask), dw[0], f32x4{0.f, 0.f, 0.f, 0.f});
        acc = mfma16(sel_frag(dup_hi(h0), mask), dw[1], acc);
        acc = mfma16(sel_frag(dup_lo(h1), mask), dw[2], acc);
        acc = mfma16(sel_frag(dup_hi(h1), mask), dw[3], acc);
        const v4u fv = {min(__float_as_uint(acc.x), 0xfffffffeu), min(__float_as_uint(acc.y), 0xfffffffeu), min(__float_as_uint(acc.z), 0xfffffffeu), min(__float_as_uint(acc.w), 0xfffffffeu)};
        if (lane < 32) __builtin_amdgcn_raw_buffer_store_b128(fv, rs, WS_XF + fu * 2048 + w * 512 + lane * 16, 0, SC1);
        STAMP(10);
      } else {
        BAR();
        flag();
        const int fe4 = ((g & 1) << 1) | (g >> 1), ocol = 128 * (rr >> 2) + 8 * (8 * (rr & 1) + (lane & 7)) + 4 * ((rr >> 1) & 1) + fe4;
        const float xres = lo_f(*(const GAS u16*)(x + ocol));
        unsigned ts;
        {
          v4u ha, hc;
          POLL_BEGIN()
            ha = __builtin_amdgcn_raw_buffer_load_b128(rs, WS_HT + lane * 1024, 0, SC1);
            hc = __builtin_amdgcn_raw_buffer_load_b128(rs, WS_HT + lane * 1024, 65536, SC1);
            ok = (ha.y == tag.x) & (hc.y == tag.x) & (ha.w == tag.y) & (hc.w == tag.y);
          POLL_END(8)
          int d = min(max(max((int)(ha.x - t0), (int)(hc.x - t0)), 0), 100000);
          d = max(d, __builtin_amdgcn_update_dpp(d, d, 0x121, 0xf, 0xf, false));
          d = max(d, __builtin_amdgcn_update_dpp(d, d, 0x122, 0xf, 0xf, false));
          d = max(d, __builtin_amdgcn_update_dpp(d, d, 0x124, 0xf, 0xf, false));
          d = max(d, __builtin_amdgcn_update_dpp(d, d, 0x128, 0xf, 0xf, false));
          ts = t0 + (unsigned)max(max(__builtin_amdgcn_readlane(d, 0), __builtin_amdgcn_readlane(d, 16)), max(__builtin_amdgcn_readlane(d, 32), __builtin_amdgcn_readlane(d, 48)));
        }
        int so = (32 * w + (lane >> 3)) * 2048 + rr * 128 + (lane & 7) * 16, fo = w == 3 ? 0x7ffffff0 - 65536 - WS_XF : so;
        unsigned swa = lb + L_FIN + lane * 16 + w * 4;
        asm volatile("" : "+v"(fo), "+v"(so), "+v"(swa));
        auto fold = [&](const v4u (&v)[4]) {
          const f32x4 a = (__builtin_bit_cast(f32x4, v[0]) + __builtin_bit_cast(f32x4, v[1])) + (__builtin_bit_cast(f32x4, v[2]) + __builtin_bit_cast(f32x4, v[3]));
          auto p0 = __builtin_amdgcn_permlane16_swap(__float_as_uint(a.x), __float_as_uint(a.z), false, false);
          auto p1 = __builtin_amdgcn_permlane16_swap(__float_as_uint(a.y), __float_as_uint(a.w), false, false);
          auto p2 = __builtin_amdgcn_permlane32_swap(__float_as_uint(__uint_as_float(p0[0]) + __uint_as_float(p0[1])),
                                                     __float_as_uint(__uint_as_float(p1[0]) + __uint_as_float(p1[1])), false, false);
          const float t = __uint_as_float(p2[0]) + __uint_as_float(p2[1]);
          return t + dpp<0x128>(t);
        };
        PAGE_START();
        wait_until(ts + FF_AT);
        POLL_BEGIN()
          const v2u ma = __builtin_amdgcn_raw_buffer_load_b64(rs, WS_ML + lane * 1024, 0, SC1), mb = __builtin_amdgcn_raw_buffer_load_b64(rs, WS_ML + lane * 1024, 65536, SC1);
          ok = (ma.y == tag.x) & (mb.y == tag.x);
        POLL_END(0)
        ts = rt();
        STAMP(10);
        wait_until(ts + XF_WAIT);
        bool need_f = true, need_s = true;
        _Pragma("nounroll") for (unsigned sp = SPIN_CAP; sp && (need_f || need_s); --sp) {
          FENCE();
          const bool go_s = need_s && (!need_f || (int)(rt() - (ts + XS_AT)) >= 0);
          if (!need_f) wait_until(ts + XS_AT);
          v4u v[4], f[4];
          if (go_s) {
#pragma unroll
            for (int i = 0; i < 4; ++i) v[i] = __builtin_amdgcn_raw_buffer_load_b128(rs, so, WS_XS + i * 16384, SC1);
          }
          if (need_f) {
#pragma unroll
            for (int i = 0; i < 4; ++i) f[i] = __builtin_amdgcn_raw_buffer_load_b128(rs, i < 2 ? so : fo, WS_XF + i * 16384, SC1);
            if (__builtin_amdgcn_ballot_w64(!full4(f)) == 0) {
              *lp<float>(swa + 1024) = fold(f);
              need_f = false;
              STAMP(11);
            }
          }
          if (go_s && __builtin_amdgcn_ballot_w64(!full4(v)) == 0) {
            *lp<float>(swa) = fold(v);
            need_s = false;
            STAMP(14);
          }
        }
        BAR();
        if (w == 0) {
          const f32x4 b = *lp<f32x4>(swa + 1024), a = *lp<f32x4>(swa);
          const float f = rbf((b.x + b.y) + (b.z + b.w));
          const float gt = rbf(f * __builtin_amdgcn_rcpf(1.0f + __builtin_amdgcn_exp2f(-LOG2E * f)));
          const float o = xres + rbf(rbf((a.x + a.y) + (a.z + a.w)) * gt);
          if ((lane & 8) == 0) *(GAS u16*)(out + ocol) = (u16)pk_bf16(o, o);
          if (rr == 0 && lane == 0) {
            const u64 e1 = (((u64)ep.y << 32) | ep.x) + 1;
            __builtin_amdgcn_raw_buffer_store_b64(v2u{(unsigned)e1, (unsigned)(e1 >> 32)}, rs, 0, 0, SC1);
          }
        }
        STAMP(13);
      }
      STAMP_OUT();
    }
  } else {
    const int h = xc + 8 * (r >> 2), j = r & 3;
    constexpr int T_QP = 0, T_QB = 2048, T_W = 3072, T_MM = 4096, T_UR = 4160, T_CL = 5120, T_CT = 6144, T_OK = 6656, T_UK = 8192;
    if (w >= 4) {
      const int u = w - 4;
      v2u ep = __builtin_amdgcn_raw_buffer_load_b64(rs, 0, 0, SC1);
      if (u < 2) *lp<v4u>(lb + T_MM + u * 1024 + lane * 16) = ones;
      wait_until(t0 + UK_DELAY);
      {
        const v4u srd = make_srd(wuk, 512u * 8192u);
#pragma unroll
        for (int i = 0; i < 8; ++i)
          dma16<POL & 4>(lb + T_UK + u * 8192 + i * 1024, dma_voff(i & 3, 8192), srd, (128 * j + 32 * u + 8 * (i & 3) + 4 * (i >> 2)) * 8192 + h * 256);
      }
      STAMP(1);
      BAR();
      asm volatile("" : "+v"(ep));
      const v2u tag = get_tag(ep);
      VMWAIT(0);
      const int gp = ((g & 1) << 1) | (g >> 1), f16 = ((n & 7) << 1) | (n >> 3);
      v4u a[2][4];
#pragma unroll
      for (int t = 0; t < 2; ++t)
#pragma unroll
        for (int s = 0; s < 4; ++s) a[t][s] = *lp<v4u>(lb + T_UK + u * 8192 + (16 * t + n) * 256 + 16 * ((4 * s + gp) ^ f16));
      STAMP(2);
      __builtin_amdgcn_s_setprio(3);
      BAR();
      f32x4 c0 = {0.f, 0.f, 0.f, 0.f}, c1 = c0;
#pragma unroll
      for (int s = 0; s < 4; ++s) {
        const v4u qf = *lp<v4u>(lb + T_QB + (4 * s + gp) * 16);
        c0 = mfma16(a[0][s], qf, c0);
        c1 = mfma16(a[1][s], qf, c1);
      }
      if (n == 0) {
        const v4u v = {min(pk_bf16(c0.x, c0.y), 0xfffffffeu), min(pk_bf16(c0.z, c0.w), 0xfffffffeu), min(pk_bf16(c1.x, c1.y), 0xfffffffeu),
                       min(pk_bf16(c1.z, c1.w), 0xfffffffeu)};
        __builtin_amdgcn_raw_buffer_store_b128(v, rs, WS_X2 + h * 1024 + j * 256 + u * 64 + g * 16, 0, SC1);
      }
      STAMP(3);
      const unsigned tr = __builtin_amdgcn_readfirstlane(*lp<unsigned>(lb + T_QB + 256));
      v4u mask = get_mask();
      int po = WS_X3 + (h * 4 + j) * 32768 + (32 * u + 8 * g) * 256 + n * 16, mo = (32 * u + (lane & 31)) * 1024 + xc * 128;
      unsigned ta[2], uo[4];
      {
        const int row = 4 * g + (n >> 2), p = n & 3, fr = ((row & 7) << 1) | (row >> 3);
#pragma unroll
        for (int t = 0; t < 2; ++t) ta[t] = lb + T_UK + row * 256 + 8 * (p & 1) + 16 * ((4 * u + 2 * t + (p >> 1)) ^ fr);
#pragma unroll
        for (int i = 0; i < 4; ++i) uo[i] = dma_voff(i, 131072);
      }
      unsigned wwa = lb + T_W + u * 128 + (lane & 31) * 4, wra = lb + T_W + u * 128 + g * 32, cwa = lb + T_CL + u * 256 + (8 * n + 4 * (g & 1)) * 2,
               cra = lb + T_CL + (n & 3) * 256 + g * 16, twa = lb + T_CT + u * 64 + n * 4;
      asm volatile("" : "+v"(mask), "+v"(po), "+v"(mo), "+v"(ta[0]), "+v"(ta[1]), "+v"(uo[0]), "+v"(uo[1]), "+v"(uo[2]), "+v"(uo[3]));
      asm volatile("" : "+v"(wwa), "+v"(wra), "+v"(cwa), "+v"(cra), "+v"(twa));
      __builtin_amdgcn_s_barrier();
      wait_until(tr + UV_OFF);
      {
        const v4u srd = make_srd(wuv, 512u * 8192u);
#pragma unroll
        for (int i = 0; i < 8; ++i) dma16<POL & 8>(lb + T_UK + u * 8192 + i * 1024, uo[i & 3], srd, (128 * j + 64 * (i >> 2) + 4 * u + (i & 3)) * 8192 + h * 256);
      }
      STAMP(4);
      wait_until(tr + ML_AT);
      unsigned ts;
      {
        v2u hv;
        POLL_BEGIN()
          hv = __builtin_amdgcn_raw_buffer_load_b64(rs, mo + WS_HT, 0, SC1);
          ok = hv.y == tag.x;
        POLL_END(8)
        int d = min(max((int)(hv.x - tr), 0), 100000);
        d = max(d, __builtin_amdgcn_update_dpp(d, d, 0x121, 0xf, 0xf, false));
        d = max(d, __builtin_amdgcn_update_dpp(d, d, 0x122, 0xf, 0xf, false));
        d = max(d, __builtin_amdgcn_update_dpp(d, d, 0x124, 0xf, 0xf, false));
        d = max(d, __builtin_amdgcn_update_dpp(d, d, 0x128, 0xf, 0xf, false));
        ts = tr + (unsigned)max(__builtin_amdgcn_readlane(d, 0), __builtin_amdgcn_readlane(d, 16));
      }
      STAMP(11);
      if (REPF > 1) {
        const __amdgpu_buffer_rsrc_t rc = __builtin_amdgcn_make_buffer_rsrc((void*)pc0, 0, CODE_BYTES, 0x00020000);
        const v4u cd = __builtin_amdgcn_raw_buffer_load_b128(rc, (r + 16 * u) * 1024 + lane * 16, 0, SC1);
        asm volatile("" ::"v"(cd));
      }
      PAGE_START();
      wait_until(ts + FF_AT);
      v4u ml;
      POLL_BEGIN()
        ml = __builtin_amdgcn_raw_buffer_load_b128(rs, mo + WS_ML + (r >> 2) * 16, 0, SC1);
        ok = (ml.y == tag.x) & (ml.w == tag.y);
      POLL_END(0)
      STAMP(5);
      v4u pay[8];
#pragma unroll
      for (int i = 0; i < 8; ++i) pay[i] = __builtin_amdgcn_raw_buffer_load_b128(rs, po + i * 256, 0, SC1);
      SB();
      const float mi = __builtin_canonicalizef(__uint_as_float(ml.x));
      float Mu = fmaxf(mi, dpp<0x121>(mi));
      Mu = fmaxf(Mu, dpp<0x122>(Mu)); Mu = fmaxf(Mu, dpp<0x124>(Mu)); Mu = fmaxf(Mu, dpp<0x128>(Mu));
      {
        auto t = __builtin_amdgcn_permlane16_swap(__float_as_uint(Mu), __float_as_uint(Mu), false, false);
        Mu = fmx(__uint_as_float(t[0]), __uint_as_float(t[1]));
      }
      const float ew = __builtin_amdgcn_exp2f(mi - Mu);
      const unsigned wd2 = pk_bf16(ew, ew);
      *lp<unsigned>(wwa) = wd2;
      FENCE();
      const v4u wa = *lp<v4u>(wra), wb = *lp<v4u>(wra + 16);
      float Lu = lo_f(wd2) * __uint_as_float(ml.z);
      Lu += dpp<0x121>(Lu); Lu += dpp<0x122>(Lu); Lu += dpp<0x124>(Lu); Lu += dpp<0x128>(Lu);
      {
        auto t = __builtin_amdgcn_permlane16_swap(__float_as_uint(Lu), __float_as_uint(Lu), false, false);
        Lu = __uint_as_float(t[0]) + __uint_as_float(t[1]);
      }
      *lp<unsigned>(lb + T_MM + u * 4) = min(__float_as_uint(Mu), 0xfffffffeu);
      *lp<unsigned>(lb + T_MM + 16 + u * 4) = min(__float_as_uint(Lu), 0xfffffffeu);
      v4u af[8];
#pragma unroll
      for (int i = 0; i < 8; ++i) { af[i] = sel_frag(i < 4 ? wa[i & 3] : wb[i & 3], mask); asm volatile("" : "+v"(af[i])); }
      v4u mm, ll;
      _Pragma("nounroll") for (unsigned sp = SPIN_CAP * 16; sp; --sp) {
        FENCE();
        mm = *lp<v4u>(lb + T_MM);
        ll = *lp<v4u>(lb + T_MM + 16);
        if (__builtin_amdgcn_ballot_w64(!(full(mm) & full(ll))) == 0) break;
      }
      FENCE();
      STAMP(6);
      v4u ub[4][2];
#pragma unroll
      for (int t = 0; t < 2; ++t)
#pragma unroll
        for (int s = 0; s < 4; ++s) {
          const v2u lo = __builtin_bit_cast(v2u, __builtin_amdgcn_ds_read_tr16_b64_v4i16(lp<s16x4>(ta[t] + s * 8192)));
          const v2u hi = __builtin_bit_cast(v2u, __builtin_amdgcn_ds_read_tr16_b64_v4i16(lp<s16x4>(ta[t] + s * 8192 + 4096)));
          ub[s][t] = v4u{lo.x, lo.y, hi.x, hi.y};
        }
      f32x4 acc = {0.f, 0.f, 0.f, 0.f};
#pragma unroll
      for (int i = 0; i < 8; ++i) acc = mfma16(af[i], pay[i], acc);
      {
        const f32x4 m4 = __builtin_bit_cast(f32x4, mm), l4 = __builtin_bit_cast(f32x4, ll);
        const float M = fmx(fmx(fmx(m4.x, m4.y), m4.z), m4.w);
        const float Lt = (__builtin_amdgcn_exp2f(m4.x - M) * l4.x + __builtin_amdgcn_exp2f(m4.y - M) * l4.y) +
                         (__builtin_amdgcn_exp2f(m4.z - M) * l4.z + __builtin_amdgcn_exp2f(m4.w - M) * l4.w);
        const float sc = __builtin_amdgcn_exp2f(Mu - M) * __builtin_amdgcn_rcpf(Lt);
        *lp<v2u>(cwa) = v2u{min(pk_bf16(acc.x * sc, acc.y * sc), 0xfffffffeu), min(pk_bf16(acc.z * sc, acc.w * sc), 0xfffffffeu)};
      }
      STAMP(7);
      v4u cf[4];
      _Pragma("nounroll") for (unsigned sp = SPIN_CAP * 16; sp; --sp) {
        FENCE();
#pragma unroll
        for (int s = 0; s < 4; ++s) cf[s] = *lp<v4u>(cra + s * 64);
        if (__builtin_amdgcn_ballot_w64(!full4(cf)) == 0) break;
      }
      f32x4 d0 = {0.f, 0.f, 0.f, 0.f}, d1 = d0;
#pragma unroll
      for (int s = 0; s < 4; ++s) { d0 = mfma16(cf[s], ub[s][0], d0); d1 = mfma16(cf[s], ub[s][1], d1); }
      *lp<unsigned>(twa) = pk_bf16((d0.x + d0.y) + (d0.z + d0.w), (d1.x + d1.y) + (d1.z + d1.w));
      STAMP(8);
      BAR();
      STAMP_OUT();
    } else {
      v4u a[8];
      const v4u xf = *(const GAS v4u*)(x + 8 * lane);
      const v4u xo = *(const GAS v4u*)(x + 128 * j + 32 * w + 8 * g);
      const v2u ep = __builtin_amdgcn_raw_buffer_load_b64(rs, 0, 0, SC1);
      SB();
      {
        const __amdgpu_buffer_rsrc_t rq = __builtin_amdgcn_make_buffer_rsrc((void*)wq, 0, 512 * 8192, 0x00020000);
#pragma unroll
        for (int i = 0; i < 8; ++i) a[i] = __builtin_amdgcn_raw_buffer_load_b128(rq, g * 65536 + n * 16, (128 * j + 32 * w + i) * 8192 + h * 256, (POL & 16) ? 2 : 0);
      }
      SB();
      const v4u mask = get_mask();
      float mean, den;
      ln_stats(xf, mean, den);
      const v2u tag = get_tag(ep);
      v4u af[8];
#pragma unroll
      for (int i = 0; i < 4; ++i) {
        const unsigned pr = ln_pair(xo[i], mean, den);
        af[2 * i] = sel_frag(dup_lo(pr), mask);
        af[2 * i + 1] = sel_frag(dup_hi(pr), mask);
      }
      STAMP(1);
      f32x4 acc = {0.f, 0.f, 0.f, 0.f};
#pragma unroll
      for (int i = 0; i < 8; ++i) acc = mfma16(af[i], a[i], acc);
      if (lane < 32) *lp<f32x4>(lb + T_QP + (w * 128 + 8 * n + 4 * g) * 4) = acc;
      STAMP(2);
      BAR();
      if (w == 0) {
        f32x2 s = *lp<f32x2>(lb + T_QP + lane * 8);
#pragma unroll
        for (int i = 1; i < 4; ++i) s += *lp<f32x2>(lb + T_QP + i * 512 + lane * 8);
        const int xo1 = WS_X1 + h * 4096 + lane * 16;
        __builtin_amdgcn_raw_buffer_store_b128(v4u{__float_as_uint(s.x), tag.x, __float_as_uint(s.y), tag.y}, rs, xo1 + j * 1024, 0, SC1);
        STAMP(3);
        wait_until(rt() + X1_WAIT);
        v4u v[4];
        POLL_BEGIN()
#pragma unroll
          for (int i = 0; i < 4; ++i) v[i] = __builtin_amdgcn_raw_buffer_load_b128(rs, xo1 + i * 1024, 0, SC1);
#pragma unroll
          for (int i = 0; i < 4; ++i) ok &= (v[i].y == tag.x) & (v[i].w == tag.y);
        POLL_END(0)
        const float q0 = (__uint_as_float(v[0].x) + __uint_as_float(v[1].x)) + (__uint_as_float(v[2].x) + __uint_as_float(v[3].x));
        const float q1 = (__uint_as_float(v[0].z) + __uint_as_float(v[1].z)) + (__uint_as_float(v[2].z) + __uint_as_float(v[3].z));
        *lp<unsigned>(lb + T_QB + lane * 4) = pk_bf16(q0, q1);
        STAMP(4);
      } else if (w == 1) {
        wait_until(t0 + 150);
        *lp<unsigned>(lb + T_QB + 256) = wait_ready(tag);
        STAMP(10);
      }
      BAR();
      const unsigned tr = __builtin_amdgcn_readfirstlane(*lp<unsigned>(lb + T_QB + 256));
      {
        __builtin_amdgcn_s_sleep(8);
        const v4u srd = make_srd(ckv, 16384u * 1024u);
        _Pragma("nounroll") for (int k = 7; k >= 8 - HELP && (int)(rt() - tr) < HELP_END; --k) {
          const int s = ((r + 4 * (HMIX == 1 ? w : HMIX == 2 ? w + k : 0)) & 15) * 8 + xc;
#pragma unroll
          for (int q = 0; q < 4; ++q) { VMWAIT(HINFL - 1); dma16<POL & 2>(lb + 98304 + w * 1024, lane * 16, srd, PAGE(k) * 4096 + q * 1024); }
        }
      }
      STAMP(9);
      wait_until(tr + TAIL_AT);
      STAMP(5);
      v4u bf[4][8];
      {
        v4u raw[4][8];
        const __amdgpu_buffer_rsrc_t ro = __builtin_amdgcn_make_buffer_rsrc((void*)wo, 0, 4096 * 1024, 0x00020000);
#pragma unroll
        for (int t = 0; t < 4; ++t) {
#pragma unroll
          for (int i = 0; i < 8; ++i) {
            const int so = (h * 128 + 32 * ((j + t) & 3) + 16 * (i & 1) + (i >> 1)) * 1024 + w * 256;
            if (t == 0) raw[t][i] = __builtin_amdgcn_raw_buffer_load_b128(ro, g * 4096 + n * 16, so, (POL & 32) ? 2 : 0);
            else raw[t][i] = __builtin_amdgcn_raw_buffer_load_b128(ro, g * 4096 + n * 16, so, (POL & 64) ? 2 : 0);
          }
          if (t == UVPOS) { SB(); FENCE(); __builtin_amdgcn_s_barrier(); FENCE(); SB(); }
        }
        SB();
#pragma unroll
        for (int t = 0; t < 4; ++t) transpose8(raw[t], bf[t]);
      }
#pragma unroll
      for (int t = 0; t < 4; ++t)
#pragma unroll
        for (int c = 0; c < 8; ++c) asm volatile("" : "+v"(bf[t][c]));
      unsigned ca[4];
#pragma unroll
      for (int t = 0; t < 4; ++t) { ca[t] = lb + T_CT + ((j + t) & 3) * 64 + g * 16; asm volatile("" : "+v"(ca[t])); }
      int xso = WS_XS + (h * 4 + j) * 2048 + w * 512 + lane * 16;
      asm volatile("" : "+v"(xso));
      STAMP(6);
      BAR();
      STAMP(7);
      v4u cf[4], sv;
#pragma unroll
      for (int t = 0; t < 4; ++t) cf[t] = *lp<v4u>(ca[t]);
      SB();
      f32x4 o[8];
#pragma unroll
      for (int c = 0; c < 8; ++c) MF0(o[c], cf[0], bf[0][c]);
#pragma unroll
      for (int k = 8; k < 32; ++k) MF(o[k & 7], cf[k >> 3], bf[k >> 3][k & 7]);
      asm volatile("s_nop 7" : "+v"(o[0]), "+v"(o[1]), "+v"(o[2]), "+v"(o[3]), "+v"(o[4]), "+v"(o[5]), "+v"(o[6]), "+v"(o[7]));
#pragma unroll
      for (int e = 0; e < 4; ++e) sv[e] = min(__float_as_uint((g & 1) ? o[4 + e].x : o[e].x), 0xfffffffeu);
      if (lane < 32) __builtin_amdgcn_raw_buffer_store_b128(sv, rs, xso, 0, SC1);
      STAMP(8);
      STAMP_OUT();
    }
  }
}

extern "C" void kernel_launch(void* const* d_in, const int* in_sizes, int n_in, void* d_out, int out_size, void* d_ws, size_t ws_size,
                              hipStream_t stream) {
  (void)hipFuncSetAttribute((const void*)fused_decode, hipFuncAttributeMaxDynamicSharedMemorySize, 163840);
  fused_decode<<<256, 512, 163840, stream>>>((const u16*)d_in[0], (const u16*)d_in[1], (const u16*)d_in[2], (const u16*)d_in[3],
                                             (const u16*)d_in[4], (const u16*)d_in[5], (const u16*)d_in[7], (const u16*)d_in[8],
                                             (const u16*)d_in[9], (u16*)d_out, (unsigned char*)d_ws + (WSOFF) KPASS);
}
```
